```python
import torch

def _softmax(x, axis=-1):
    return torch.softmax(x, dim=axis)
def _gelu(x):
    return torch.nn.functional.gelu(x, approximate="tanh")
def _silu(x):
    return torch.nn.functional.silu(x)
def _sigmoid(x):
    return torch.sigmoid(x)
def _layernorm(x, axis=-1, eps=1e-5):
    m = x.mean(dim=axis, keepdim=True)
    v = x.var(dim=axis, keepdim=True, unbiased=False)
    return (x - m) / torch.sqrt(v + eps)
def _scatter_add(tgt, idx, src, axis=0):
    return tgt.clone().index_add_(axis, idx, src)
def _segment_sum(src, seg, n):
    out = torch.zeros((n,) + src.shape[1:], dtype=src.dtype)
    return out.index_add_(0, seg, src)
def _pad(x, pw):
    flat = tuple(v for pair in reversed(pw) for v in pair)
    return torch.nn.functional.pad(x, flat)

kernel_name = 'interleave_layers__sliding_local_dilated__moe_arch_mod__prompt'

def setup_inputs():

    _w = (lambda a: (lambda t: t.to(torch.bfloat16) if t.is_floating_point() else t)(torch.as_tensor(a)))
    def _A_setup():
        B, S, D = 1, 2048, 512
        return {
            "x": _w((lambda *s: torch.randn(*s, dtype=torch.bfloat16) * 0.05)(B, S, D)),
            "wqkv": _w((lambda *s: torch.randn(*s, dtype=torch.bfloat16) * 0.05)(D, 3 * D)),
            "wo": _w((lambda *s: torch.randn(*s, dtype=torch.bfloat16) * 0.05)(D, D)),
        }
    _A_in = _A_setup()

    def _B_setup():
        B, S, D, F, C = 1, 2048, 512, 1792, 8
        return {
            "x": _w((lambda *s: torch.randn(*s, dtype=torch.bfloat16) * 0.05)(B, S, D)),
            "w_r": _w((lambda *s: torch.randn(*s, dtype=torch.bfloat16) * 0.05)(D)),
            "w_gate": _w((lambda *s: torch.randn(*s, dtype=torch.bfloat16) * 0.05)(D, F)),
            "w_up": _w((lambda *s: torch.randn(*s, dtype=torch.bfloat16) * 0.05)(D, F)),
            "w_down": _w((lambda *s: torch.randn(*s, dtype=torch.bfloat16) * 0.05)(F, D)),
        }
    _B_in = _B_setup()

    return {
        "A_x": _A_in["x"],
        "A_wqkv": _A_in["wqkv"],
        "A_wo": _A_in["wo"],
        "B_x": _B_in["x"],
        "B_w_r": _B_in["w_r"],
        "B_w_gate": _B_in["w_gate"],
        "B_w_up": _B_in["w_up"],
        "B_w_down": _B_in["w_down"],
    }

def reference(A_x, A_wqkv, A_wo, B_x, B_w_r, B_w_gate, B_w_up, B_w_down):
    def _A_ref(x, wqkv, wo):
        B, S, D = x.shape
        H = 32; Dh = D // H
        W, dil = 8, 2
        qkv = (lambda x, *d: x.permute(*d))((x @ wqkv).reshape(B, S, 3, H, Dh), 2, 0, 3, 1, 4)
        q, k, v = qkv[0], qkv[1], qkv[2]
        scale = Dh ** -0.5
        pos = torch.arange(S)
        off = torch.arange(W) * dil
        idx = pos[:, None] - off[None, :]
        mask = idx >= 0
        idx_c = torch.where(mask, idx, 0).reshape(-1)
        kg = k[:, :, idx_c, :].reshape(B, H, S, W, Dh)
        vg = v[:, :, idx_c, :].reshape(B, H, S, W, Dh)
        logits = torch.einsum("bhsd,bhswd->bhsw", q, kg) * scale
        logits = torch.where(mask[None, None, :, :], logits, -1e9)
        attn = _softmax(logits, axis=-1)
        o = torch.einsum("bhsw,bhswd->bhsd", attn, vg)
        return (lambda x, *d: x.permute(*d))(o, 0, 2, 1, 3).reshape(B, S, D) @ wo

    def _B_ref(x, w_r, w_gate, w_up, w_down):
        B, S, D = x.shape
        C = 8
        r = x @ w_r
        idx = torch.argsort(-r, axis=-1)[:, :C]
        idx_e = (lambda x, n, axis: torch.repeat_interleave(x, n, dim=axis))(idx.reshape(B, C, 1), D, axis=2)
        x_sel = (lambda x, i, axis: torch.take_along_dim(x, i, dim=axis))(x, idx_e, axis=1)
        g = _silu(x_sel @ w_gate)
        u = x_sel @ w_up
        y = (g * u) @ w_down
        r_sel = _sigmoid((lambda x, i, axis: torch.take_along_dim(x, i, dim=axis))(r, idx, axis=1))
        y = y * r_sel.reshape(B, C, 1)
        b_off = (torch.arange(B) * S).reshape(B, 1)
        flat_idx = (idx + b_off).reshape(B * C)
        zero = (x * 0.0).reshape(B * S, D)
        upd = _scatter_add(zero, flat_idx, y.reshape(B * C, D), axis=0)
        return x + upd.reshape(B, S, D)

    x = A_x
    x = x + _A_ref(_layernorm(x, axis=-1), A_wqkv, A_wo)
    x = x + _B_ref(_layernorm(x, axis=-1), B_w_r, B_w_gate, B_w_up, B_w_down)
    x = x + _A_ref(_layernorm(x, axis=-1), A_wqkv, A_wo)
    x = x + _B_ref(_layernorm(x, axis=-1), B_w_r, B_w_gate, B_w_up, B_w_down)
    return x
```

```cpp
#include <hip/hip_runtime.h>
#include <hip/hip_bf16.h>
#include <stdint.h>

typedef unsigned short u16;
typedef unsigned int u32;
typedef unsigned long long u64;
typedef u32 v4u __attribute__((ext_vector_type(4)));
typedef u32 v2u __attribute__((ext_vector_type(2)));
typedef float f32x4 __attribute__((ext_vector_type(4)));
typedef float f32x2 __attribute__((ext_vector_type(2)));
typedef short bf16x8 __attribute__((ext_vector_type(8)));
typedef short s16x4 __attribute__((ext_vector_type(4)));
typedef __bf16 bf16x2_t __attribute__((ext_vector_type(2)));
#define LAS __attribute__((address_space(3)))
#define GAS __attribute__((address_space(1)))
#define SCHED_FENCE() __builtin_amdgcn_sched_barrier(0)

constexpr int S_ = 2048, D_ = 512, F_ = 1792;

__device__ __forceinline__ float bf_lo(u32 p) { return __uint_as_float(p << 16); }
__device__ __forceinline__ float bf_hi(u32 p) { return __uint_as_float(p & 0xffff0000u); }
__device__ __forceinline__ u32 pack2(float a, float b) {
  bf16x2_t v = {(__bf16)a, (__bf16)b};
  return __builtin_bit_cast(u32, v);
}
__device__ __forceinline__ float rbf(float a) { return (float)(__bf16)a; }
__device__ __forceinline__ float dot2(u32 a, u32 b, float acc) {
  return __builtin_amdgcn_fdot2_f32_bf16(__builtin_bit_cast(bf16x2_t, a), __builtin_bit_cast(bf16x2_t, b), acc, false);
}
__device__ __forceinline__ f32x4 mfma16(v4u a, v4u b, f32x4 c) {
  return __builtin_amdgcn_mfma_f32_16x16x32_bf16(__builtin_bit_cast(bf16x8, a), __builtin_bit_cast(bf16x8, b), c, 0, 0, 0);
}
__device__ __forceinline__ v2u tr_read(const LAS unsigned char* p) {
  return __builtin_bit_cast(v2u, __builtin_amdgcn_ds_read_tr16_b64_v4i16((LAS s16x4*)p));
}
__device__ __forceinline__ v4u wfrag_at(const LAS unsigned char* a) {
  v2u lo = tr_read(a), hi = tr_read(a + 256);
  v4u r; r.x = lo.x; r.y = lo.y; r.z = hi.x; r.w = hi.y;
  return r;
}
__device__ __forceinline__ int wf_lane_off(int lane, int nt2) {
  const int g = lane >> 4, q = (lane >> 2) & 3, p = lane & 3;
  return g * 512 + q * 64 + ((nt2 ^ (g & 1)) * 32) + p * 8;
}

template <int CTRL> __device__ __forceinline__ float dpp_f(float v) {
  return __int_as_float(__builtin_amdgcn_update_dpp(0, __float_as_int(v), CTRL, 0xf, 0xf, true));
}
template <int CTRL> __device__ __forceinline__ u32 dpp_u(u32 v) {
  return (u32)__builtin_amdgcn_update_dpp(0, (int)v, CTRL, 0xf, 0xf, true);
}
constexpr int DPP_XOR1 = 0xB1, DPP_XOR2 = 0x4E, DPP_HMIRROR = 0x141, DPP_MIRROR = 0x140, DPP_ROR8 = 0x128;
__device__ __forceinline__ float sum16(float v) {
  v += dpp_f<DPP_XOR1>(v); v += dpp_f<DPP_XOR2>(v); v += dpp_f<DPP_HMIRROR>(v); v += dpp_f<DPP_MIRROR>(v);
  return v;
}
__device__ __forceinline__ float xor16_add(float v) {
  const u32 a = __float_as_uint(v);
  v2u r = __builtin_amdgcn_permlane16_swap(a, a, false, false);
  const u32 rx = r.x, ry = r.y;
  return __uint_as_float(rx) + __uint_as_float(ry);
}
__device__ __forceinline__ float xor32_add(float v) {
  const u32 a = __float_as_uint(v);
  v2u r = __builtin_amdgcn_permlane32_swap(a, a, false, false);
  const u32 rx = r.x, ry = r.y;
  return __uint_as_float(rx) + __uint_as_float(ry);
}
__device__ __forceinline__ float sum64(float v) { return xor32_add(xor16_add(sum16(v))); }
__device__ __forceinline__ u32 max16u(u32 v) {
  v = max(v, dpp_u<DPP_XOR1>(v)); v = max(v, dpp_u<DPP_XOR2>(v)); v = max(v, dpp_u<DPP_HMIRROR>(v)); v = max(v, dpp_u<DPP_MIRROR>(v));
  return v;
}
__device__ __forceinline__ u32 max64u(u32 v) {
  v = max16u(v);
  { v2u r = __builtin_amdgcn_permlane16_swap(v, v, false, false); const u32 rx = r.x, ry = r.y; v = max(rx, ry); }
  { v2u r = __builtin_amdgcn_permlane32_swap(v, v, false, false); const u32 rx = r.x, ry = r.y; v = max(rx, ry); }
  return v;
}

__device__ __forceinline__ void dma16(u32 voff, const void* sbase, u32 lds_addr) {
  asm volatile("s_mov_b32 m0, %2\n\ts_nop 0\n\tglobal_load_lds_dwordx4 %0, %1" :: "v"(voff), "s"(sbase), "s"(lds_addr) : "memory");
}
#define WAIT_VM(n) asm volatile("s_waitcnt vmcnt(" #n ")" ::: "memory")
#define BAR() asm volatile("s_waitcnt lgkmcnt(0)\n\ts_barrier" ::: "memory")
#define CFENCE() asm volatile("" ::: "memory")
typedef short bf16x4s __attribute__((ext_vector_type(4)));
__device__ __forceinline__ f32x4 mfma16k16(v2u a, v2u b, f32x4 c) {
  return __builtin_amdgcn_mfma_f32_16x16x16bf16_1k(__builtin_bit_cast(bf16x4s, a), __builtin_bit_cast(bf16x4s, b), c, 0, 0, 0);
}
__device__ __forceinline__ float xor16_max(float v) {
  const u32 a = __float_as_uint(v);
  v2u r = __builtin_amdgcn_permlane16_swap(a, a, false, false);
  const u32 rx = r.x, ry = r.y;
  return fmaxf(__uint_as_float(rx), __uint_as_float(ry));
}
__device__ __forceinline__ float xor32_max(float v) {
  const u32 a = __float_as_uint(v);
  v2u r = __builtin_amdgcn_permlane32_swap(a, a, false, false);
  const u32 rx = r.x, ry = r.y;
  return fmaxf(__uint_as_float(rx), __uint_as_float(ry));
}

#include <type_traits>


constexpr u32 MBy = 1u << 20;
constexpr u32 WS_O1 = 0, WS_X1 = 2 * MBy, WS_XN2 = 6 * MBy, WS_O2 = 8 * MBy, WS_X3 = 10 * MBy, WS_SM = 14 * MBy;
constexpr u32 SM_STRIDE = 128 * 1024, SM_KEY = 0  , SM_H = 32768, SM_SEL = 61440  , SM_U = 65536, SM_RI = 98304  ;
constexpr u32 WS_PS = WS_SM + 2 * SM_STRIDE;
constexpr u32 WS_CS = WS_PS + 2 * 262144;
constexpr u32 WS_FL = WS_CS + 131072;
constexpr u32 WS_F0 = WS_FL + 32768;
constexpr u32 WS_EP = WS_F0 + 32768;
constexpr u32 WS_BYTES = WS_EP + 128;
constexpr u32 SENT = 0xFFFFFFFFu;
constexpr int SPIN_MAX = 1 << 17;
#define XH 4
#define G0 1
#define G1 3
#define G2 7
#define OUT_WT 16
#define KEY_SLEEP 2
#define O_SLEEP 2
#define POLL_SLEEP 2
#define XP 4
#define WA 6
#define WA1 7
#define WAIT_VMX_(n) WAIT_VM(n)
#define WAIT_VMX(n) WAIT_VMX_(n)
typedef __amdgpu_buffer_rsrc_t rsrc_t;
__device__ __forceinline__ v4u ld16c(rsrc_t r, u32 off) { return __builtin_amdgcn_raw_buffer_load_b128(r, off, 0, 16); }
__device__ __forceinline__ v2u ld8c(rsrc_t r, u32 off) { return __builtin_amdgcn_raw_buffer_load_b64(r, off, 0, 16); }
__device__ __forceinline__ u32 ld4c(rsrc_t r, u32 off) { return __builtin_amdgcn_raw_buffer_load_b32(r, off, 0, 16); }
__device__ __forceinline__ void st16c(rsrc_t r, u32 off, v4u v) { __builtin_amdgcn_raw_buffer_store_b128(v, r, off, 0, 16); }
__device__ __forceinline__ void st8c(rsrc_t r, u32 off, v2u v) { __builtin_amdgcn_raw_buffer_store_b64(v, r, off, 0, 16); }
__device__ __forceinline__ void st4c(rsrc_t r, u32 off, u32 v) { __builtin_amdgcn_raw_buffer_store_b32(v, r, off, 0, 16); }
__device__ __forceinline__ u32 max4(v4u v) { return max(max(v.x, v.y), max(v.z, v.w)); }
__device__ __forceinline__ bool ok4(v4u v) { return max4(v) != SENT; }
#define PIN4(v) asm volatile("" : "+v"(v))
__device__ __forceinline__ bool wave_all(bool p) { return __builtin_amdgcn_ballot_w64(!p) == 0ull; }
__device__ __forceinline__ v4u poll16(rsrc_t r, u32 off) {
  v4u v;
  for (int it = 0;; ++it) {
    CFENCE();
    v = ld16c(r, off);
    if (wave_all(ok4(v)) || it >= SPIN_MAX) break;
    __builtin_amdgcn_s_sleep(POLL_SLEEP);
  }
  CFENCE();
  return v;
}
__device__ __forceinline__ v4u widen16(v2u a, v2u b) {
  const v2u r0 = __builtin_amdgcn_permlane16_swap(a.x, b.x, false, false);
  const v2u r1 = __builtin_amdgcn_permlane16_swap(a.y, b.y, false, false);
  return v4u{r0.x, r1.x, r0.y, r1.y};
}

__device__ __forceinline__ v4u attn_tile(const LAS unsigned char* fr, int T, bool first, int lane) {
  const int tok = lane & 15, g = lane >> 4;
  const LAS unsigned char* own = fr + (T + 1) * 3072 + lane * 8;
  const LAS unsigned char* prv = own - 3072;
  v2u qf[2], kf[2], vf[2], kp[2], vp[2];
#pragma unroll
  for (int h = 0; h < 2; ++h) {
    qf[h] = *(const LAS v2u*)(own + h * 512); kf[h] = *(const LAS v2u*)(own + (2 + h) * 512); kp[h] = *(const LAS v2u*)(prv + (2 + h) * 512);
  }
#pragma unroll
  for (int h = 0; h < 2; ++h) { vf[h] = *(const LAS v2u*)(own + (4 + h) * 512); vp[h] = *(const LAS v2u*)(prv + (4 + h) * 512); }
  const f32x4 Z = {0.f, 0.f, 0.f, 0.f};
  const bool odd = tok & 1;
  const int i0 = 4 * g + (tok & 1);
  const bool own0 = i0 <= tok, own1 = i0 + 2 <= tok;
  f32x4 so[2], sp[2];
#pragma unroll
  for (int h = 0; h < 2; ++h) { so[h] = mfma16k16(kf[h], qf[h], Z); sp[h] = mfma16k16(kp[h], qf[h], Z); }
  float l0[2], l1[2], mx[2], e0[2], e1[2], sm[2];
#pragma unroll
  for (int h = 0; h < 2; ++h) {
    const float so0 = odd ? so[h][1] : so[h][0], so1 = odd ? so[h][3] : so[h][2];
    const float sp0 = odd ? sp[h][1] : sp[h][0], sp1 = odd ? sp[h][3] : sp[h][2];
    l0[h] = rbf(own0 ? so0 : sp0) * 0.25f; l1[h] = rbf(own1 ? so1 : sp1) * 0.25f;
    if (first) { l0[h] = own0 ? l0[h] : -1e9f; l1[h] = own1 ? l1[h] : -1e9f; }
    mx[h] = fmaxf(l0[h], l1[h]);
  }
#pragma unroll
  for (int h = 0; h < 2; ++h) mx[h] = xor16_max(mx[h]);
#pragma unroll
  for (int h = 0; h < 2; ++h) mx[h] = xor32_max(mx[h]);
#pragma unroll
  for (int h = 0; h < 2; ++h) { e0[h] = __expf(l0[h] - mx[h]); e1[h] = __expf(l1[h] - mx[h]); sm[h] = e0[h] + e1[h]; }
#pragma unroll
  for (int h = 0; h < 2; ++h) sm[h] = xor16_add(sm[h]);
#pragma unroll
  for (int h = 0; h < 2; ++h) sm[h] = xor32_add(sm[h]);
  v2u res[2];
#pragma unroll
  for (int h = 0; h < 2; ++h) {
    const float inv = __builtin_amdgcn_rcpf(sm[h]);
    const u32 p01 = pack2(e0[h] * inv, e1[h] * inv);
    const u32 p0 = odd ? (p01 << 16) : (p01 & 0xffffu), p1 = odd ? (p01 & 0xffff0000u) : (p01 >> 16);
    const v2u po = {own0 ? p0 : 0u, own1 ? p1 : 0u}, pp = {own0 ? 0u : p0, own1 ? 0u : p1};
    f32x4 oa = mfma16k16(vf[h], po, Z);
    oa = mfma16k16(vp[h], pp, oa);
    res[h] = v2u{pack2(oa[0], oa[1]), pack2(oa[2], oa[3])};
  }
  return widen16(res[0], res[1]);
}

__device__ __forceinline__ u32 of_off(int tile, int j, int lane) {
  const int tok = lane & 15, g = lane >> 4;
  return (u32)(((tile * 16 + j) * 64 + (2 * (g & 1) + (g >> 1)) * 16 + tok) * 16);
}
constexpr int B_W_OFF = 0;
constexpr int B_WO0_OFF = 98304;
constexpr int B_WO1_OFF = 0, B_EX1_OFF = 65536;
constexpr int B_FX_OFF = 98304;
constexpr int B_TR_OFF = 98304;
constexpr int B_HX_OFF = 98304;
constexpr int B_RED_OFF = 114688;
constexpr int B_FR_OFF = 131072;
constexpr int B_P_OFF = 158720;
constexpr int P_SEL = B_P_OFF, P_WM = B_P_OFF + 64, P_CNT = B_P_OFF + 128, P_TAB = B_P_OFF + 256;
constexpr int P_AB = B_P_OFF + 1024, P_CS = B_P_OFF + 2304;
constexpr int B_LDS = 163840;
constexpr int A_HX_OFF = 131072;
constexpr int A_EX_OFF = 98304;
constexpr int A_EXH_OFF = A_EX_OFF + 49152;
constexpr int A_CS_OFF = B_P_OFF;
static_assert(A_EXH_OFF + 8192 <= B_P_OFF, "lds");

__device__ __forceinline__ u32 rkey(float r, int idx) {
  u32 u = __float_as_uint(r);
  u = (u & 0x80000000u) ? ~u : (u | 0x80000000u);
  return (u & 0xffff0000u) | (u32)(2047 - idx);
}
__device__ __forceinline__ float key_val(u32 k) {
  const u32 u = k & 0xffff0000u;
  return __uint_as_float((u & 0x80000000u) ? (u & 0x7fffffffu) : (~u & 0xffff0000u));
}
__device__ __forceinline__ int key_idx(u32 k) { return 2047 - (int)(k & 2047u); }
__device__ __forceinline__ u32 st_off(int row) { return SM_RI + (u32)(row * 16); }
__device__ __forceinline__ v4u seal(u32 a, u32 b, u32 c, u32 tag) { return v4u{a, b, c, a ^ b ^ c ^ tag}; }
__device__ __forceinline__ u32 seal_err(v4u v, u32 tag) { return v.x ^ v.y ^ v.z ^ v.w ^ tag; }
__device__ __forceinline__ bool sealed(v4u v, u32 tag) { return seal_err(v, tag) == 0u; }
__device__ __forceinline__ v4u poll_sealed(rsrc_t r, u32 off, u32 tag) {
  v4u v;
  for (int it = 0;; ++it) {
    CFENCE();
    v = ld16c(r, off);
    if (wave_all(sealed(v, tag)) || it >= SPIN_MAX) break;
    __builtin_amdgcn_s_sleep(POLL_SLEEP);
  }
  CFENCE();
  return v;
}

__device__ __forceinline__ v4u norm8(v4u v, float mb, float rs) {
  v4u o;
#pragma unroll
  for (int e = 0; e < 4; ++e) { const u32 p = v[e]; const u32 d = pack2(bf_lo(p) - mb, bf_hi(p) - mb); o[e] = pack2(bf_lo(d) * rs, bf_hi(d) * rs); }
  return o;
}
__device__ __forceinline__ v4u add8(v4u a, v4u b) {
  v4u o;
#pragma unroll
  for (int e = 0; e < 4; ++e) { const u32 p = a[e], q = b[e]; o[e] = pack2(bf_lo(p) + bf_lo(q), bf_hi(p) + bf_hi(q)); }
  return o;
}
__device__ __forceinline__ v4u norm8s(v4u v, u32 st) { return norm8(v, bf_lo(st), __builtin_amdgcn_rcpf(bf_hi(st))); }
__device__ __forceinline__ u32 row_stats(v4u v) {
  float s = 0.f, q = 0.f;
#pragma unroll
  for (int e = 0; e < 4; ++e) { const u32 p = v[e]; s = dot2(p, 0x3F803F80u, s); q = dot2(p, p, q); }
  s = sum64(s); q = sum64(q);
  const float mean = s * (1.0f / D_);
  const float var = fmaxf(__builtin_fmaf(-mean, mean, q * (1.0f / D_)), 0.f);
  return pack2(mean, __builtin_amdgcn_sqrtf(rbf(rbf(var) + 1e-5f)));
}

__global__ __launch_bounds__(512, 2) void k_b(const u16* __restrict__ x, const u16* __restrict__ wqkv, const u16* __restrict__ wo, const u16* __restrict__ wr,
                                              const u16* __restrict__ wg, const u16* __restrict__ wu, const u16* __restrict__ wd, u16* __restrict__ out,
                                              unsigned char* __restrict__ ws
) {
  extern __shared__ __attribute__((aligned(16))) unsigned char smem_[];
  LAS unsigned char* lds = (LAS unsigned char*)smem_;
  const u32 lds0 = (u32)(uintptr_t)lds;
  const int tid = threadIdx.x, lane = tid & 63, w = __builtin_amdgcn_readfirstlane(tid >> 6);
  const int b = blockIdx.x, xcd = b & 7, bi = b >> 3;
  const int c2 = (xcd & 3) * 8 + (bi & 7), j2 = (xcd >> 2) * 4 + (bi >> 3);
  const int c = (xcd & 3) * 4 + (bi & 3), j = (xcd >> 2) * 8 + (bi >> 2);
  const int tok = lane & 15, g = lane >> 4, lr = lane >> 3, lc = lane & 7;
  const int m = w >> 1, kh = w & 1;
  const bool is_dn = b >= 224;
  const int fsl = xcd * 28 + bi;
  const int dsl = xcd * 4 + (bi - 28);
  const rsrc_t rs = __builtin_amdgcn_make_buffer_rsrc(ws, 0, WS_BYTES, 0x00020000);
  const v4u S4 = {SENT, SENT, SENT, SENT};
  u32 pf[4] = {0u, 0u, 0u, 0u};
  v4u fw[7];
  const int fsw = (tok >> 1) & 7;
  const int rb0 = B_W_OFF + wf_lane_off(lane, 0), rb1 = B_W_OFF + wf_lane_off(lane, 1);
  u32 tag;
  {
    const u32 ONES = 0x3F803F80u;
    const v4u ONES4 = {ONES, ONES, ONES, ONES};
    const int rg = 8 * c2 + j2;
    v4u xrow; u32 epoch;
    asm volatile("global_load_dwordx4 %0, %1, %2" : "=v"(xrow) : "v"((u32)lane * 16u), "s"(x + (size_t)(8 * rg + w) * D_) : "memory");
    asm volatile("global_load_dword %0, %1, %2 sc1" : "=v"(epoch) : "v"(0u), "s"(ws + WS_EP) : "memory");
    u32 wq_vo;
    {
      const int gl = lane >> 5, wrow = (lane >> 2) & 7, nt2 = ((lane >> 1) & 1) ^ gl, p2 = lane & 1;
      wq_vo = (u32)((8 * gl + wrow) * (3 * D_) + 16 * nt2 + 8 * p2) * 2u;
    }
    const unsigned char* wqb = (const unsigned char*)(wqkv + 32 * j + (size_t)(64 * w) * (3 * D_));
    auto wq_dma = [&](int i) {
      const int kq = i / 6, e = i % 6;
      dma16(wq_vo, wqb + (size_t)((32 * kq + 16 * (e & 1)) * (3 * D_) + (e >> 1) * D_) * 2, lds0 + B_W_OFF + (12 * w + i) * 1024);
    };
    u32 wt = 0;
    if (w < 3) {
      const int t = (w * 64 + lane) * 32 + bi;
      asm volatile("global_load_dword %0, %1, %2" : "=v"(wt) : "v"((u32)((t / 12) * (3 * D_) + ((t % 12) >> 2) * D_ + 64 * (t & 3)) * 2u), "s"(wqkv + 256 * (xcd >> 2)) : "memory");
    }
#pragma unroll
    for (int i = 0; i < WA; ++i) wq_dma(i);
    if (w < 3) WAIT_VMX(WA1); else WAIT_VMX(WA);
    asm volatile("" : "+v"(xrow), "+v"(epoch) :: "memory");

    tag = __builtin_amdgcn_readfirstlane(epoch) + 1u;
    tag = tag == 0u ? 1u : tag;
    {
      *(LAS v4u*)(lds + B_TR_OFF + w * 1040 + lane * 16) = norm8s(xrow, row_stats(xrow));
      BAR();
      const v4u v = *(const LAS v4u*)(lds + B_TR_OFF + lc * 1040 + (8 * w + lr) * 16);
      st16c(rs, WS_XN2 + (u32)((((rg >> 1) * 64 + 8 * w + lr) * 16 + (rg & 1) * 8 + lc) * 16), v);
      WAIT_VM(0);
      asm volatile("" : "+v"(wt));
      BAR();
      if (tid == 0) st4c(rs, WS_F0 + (u32)(rg * 128), tag);
    }
#pragma unroll
    for (int i = WA; i < 12; ++i) wq_dma(i);
    const int th = c == 0 ? 0 : 8 * c - 1;
    {
      const u32 fo = WS_F0 + (u32)(((lane & 4) ? 2 * th + (lane & 1) : 2 * (8 * c + 2 * m) + (lane & 3)) * 128);
      for (int it = 0;; ++it) {
        CFENCE();
        const u32 f = ld4c(rs, fo);
        if (wave_all(f == tag) || it >= SPIN_MAX) break;
        __builtin_amdgcn_s_sleep(POLL_SLEEP);
      }
      CFENCE();
    }
    v4u xl[2][8], hl[2];
    {
      const u32 ho = WS_XN2 + (u32)((th * 1024 + 128 * w + lane) * 16);
      hl[0] = ld16c(rs, ho); hl[1] = ld16c(rs, ho + 1024);
    }
    const u32 xo0 = WS_XN2 + (u32)((((8 * c + w) * 16 + 8 * kh) * 64 + lane) * 16), xo1 = WS_XN2 + (u32)((((8 * c + (w ^ 1)) * 16 + 8 * kh) * 64 + lane) * 16);
#pragma unroll
    for (int u = 0; u < XP; ++u) { xl[0][u] = ld16c(rs, xo0 + u * 1024); xl[1][u] = ld16c(rs, xo1 + u * 1024); }
    SCHED_FENCE();
    *(LAS v4u*)(lds + A_HX_OFF + w * 2048 + lane * 16) = tok == 0 ? ONES4 : hl[0];
    *(LAS v4u*)(lds + A_HX_OFF + w * 2048 + 1024 + lane * 16) = tok == 0 ? ONES4 : hl[1];
    BAR();
    st16c(rs, WS_O1 + of_off(8 * c + w, j, lane), S4);
    const int pw = w ^ 1;
    f32x4 acc[2][6];
#pragma unroll
    for (int n = 0; n < 6; ++n) { acc[0][n] = f32x4{0.f, 0.f, 0.f, 0.f}; acc[1][n] = f32x4{0.f, 0.f, 0.f, 0.f}; }
    const LAS unsigned char* wk = lds + kh * (8 * 6144);
    {
      v4u wf[2][6];
      auto wrd = [&](int ks, int n) { wf[ks & 1][n] = wfrag_at(wk + ((n & 1) ? rb1 : rb0) + ks * 6144 + (n >> 1) * 2048); };
#pragma unroll
      for (int n = 0; n < 6; ++n) wrd(0, n);
#pragma unroll
      for (int ks = 0; ks < 8; ++ks) {
#pragma unroll
        for (int n = 0; n < 6; ++n) {
          SCHED_FENCE();
          if (ks + 1 < 8) wrd(ks + 1, n);
#pragma unroll
          for (int t = 0; t < 2; ++t) {
            if (n < 4) acc[t][n] = mfma16(wf[ks & 1][n], xl[t][ks], acc[t][n]);
            else acc[t][n] = mfma16(xl[t][ks], wf[ks & 1][n], acc[t][n]);
          }
          if (ks + XP < 8) {
            if (n == 1) xl[0][ks + XP] = ld16c(rs, xo0 + (ks + XP) * 1024);
            if (n == 3) xl[1][ks + XP] = ld16c(rs, xo1 + (ks + XP) * 1024);
          }
        }
      }
      SCHED_FENCE();
    }
    WAIT_VM(0);
    {
      const int t = ((w - 3) * 64 + lane) * 32 + bi;
      const u16* p = wo;
      if (t < 2048) p = wo + (size_t)(t >> 2) * D_ + 256 * (xcd >> 2) + 64 * (t & 3);
      else if (t < 3840) p = wd + (size_t)(t - 2048) * D_ + 64 * xcd;
      else {
        const int u = t - 3840, r = (u >> 1) / 5, q = (u >> 1) % 5;
        int col = 224 * xcd + 64 * q; col = col > 224 * xcd + 222 ? 224 * xcd + 222 : col;
        p = ((u & 1) ? wu : wg) + (size_t)r * F_ + col;
      }
      if (w >= 3 && t < 8960) pf[0] = *(const u32*)p;
    }
    {
      st16c(rs, WS_O2 + of_off(8 * c + w, j, lane), S4);
      if (w < 2 && lane < 16) {
        if (!is_dn) { if (lane < 8) st16c(rs, WS_SM + w * SM_STRIDE + SM_H + fsl * 128 + lane * 16, S4); }
        else if (w == 0) st16c(rs, WS_SM + SM_U + dsl * 256 + lane * 16, S4);
      }
    }
    f32x4 ahh;
    {
      f32x4 ah0 = {0.f, 0.f, 0.f, 0.f}, ah1 = {0.f, 0.f, 0.f, 0.f};
      const LAS unsigned char* wp = wk + B_W_OFF + (1 + (m >> 1)) * 2048 + wf_lane_off(lane, m & 1);
      v4u hx[8], hwf[8];
#pragma unroll
      for (int u = 0; u < 8; ++u) { hx[u] = *(const LAS v4u*)(lds + A_HX_OFF + (8 * kh + u) * 1024 + lane * 16); hwf[u] = wfrag_at(wp + u * 6144); }
      if (m < 2) {
#pragma unroll
        for (int u = 0; u < 8; ++u) ah0 = mfma16(hwf[u], hx[u], ah0);
#pragma unroll
        for (int u = 0; u < 8; ++u) ah1 = mfma16(wfrag_at(wp - 2048 + u * 6144), hx[u], ah1);
        if (tok == 0) {
          *(LAS f32x4*)(lds + A_CS_OFF + (kh * 96 + 32 + 16 * m + 4 * g) * 4) = ah0;
          *(LAS f32x4*)(lds + A_CS_OFF + (kh * 96 + 16 * m + 4 * g) * 4) = ah1;
        }
        ahh = ah0;
      } else {
#pragma unroll
        for (int u = 0; u < 4; ++u) { ah0 = mfma16(hx[2 * u], hwf[2 * u], ah0); ah1 = mfma16(hx[2 * u + 1], hwf[2 * u + 1], ah1); }
        ahh = ah0 + ah1;
        if (g == 0) *(LAS float*)(lds + A_CS_OFF + (kh * 96 + 32 + 16 * m + tok) * 4) = ahh[0];
      }
    }
    BAR();
    if (tid == 0) st4c(rs, WS_FL + (u32)((c * 16 + j) * 128), tag);
#pragma unroll
    for (int n = 0; n < 6; ++n) *(LAS f32x4*)(lds + A_EX_OFF + ((w * 6 + n) * 64 + lane) * 16) = acc[1][n];
    if (kh == 1) *(LAS f32x4*)(lds + A_EXH_OFF + (m * 64 + lane) * 16) = ahh;
    if (w == 1) {
      *(float*)(ws + WS_CS + (b * 128 + lane) * 4) = *(const LAS float*)(lds + A_CS_OFF + lane * 4) + *(const LAS float*)(lds + A_CS_OFF + (96 + lane) * 4);
      if (lane < 32) *(float*)(ws + WS_CS + (b * 128 + 64 + lane) * 4) = *(const LAS float*)(lds + A_CS_OFF + (64 + lane) * 4) + *(const LAS float*)(lds + A_CS_OFF + (160 + lane) * 4);
    }
    BAR();
#pragma unroll
    for (int n = 0; n < 6; ++n) acc[0][n] += *(const LAS f32x4*)(lds + A_EX_OFF + ((pw * 6 + n) * 64 + lane) * 16);
    if (kh == 0) ahh += *(const LAS f32x4*)(lds + A_EXH_OFF + (m * 64 + lane) * 16);
    BAR();
    {
      LAS unsigned char* frw = lds + B_FR_OFF + (w + 1) * 3072 + lane * 8;
#pragma unroll
      for (int n = 0; n < 6; ++n) *(LAS v2u*)(frw + n * 512) = v2u{pack2(acc[0][n][0], acc[0][n][1]), pack2(acc[0][n][2], acc[0][n][3])};
      if (kh == 0) *(LAS v2u*)(lds + B_FR_OFF + (2 + m) * 512 + lane * 8) = v2u{pack2(ahh[0], ahh[1]), pack2(ahh[2], ahh[3])};
    }
    BAR();
  }
  u32 wo_off;
  {
    const int gl = lane >> 5, wrow = (lane >> 2) & 7, nt2 = ((lane >> 1) & 1) ^ gl, p2 = lane & 1;
    const int kq = w >> 2, sec = (w >> 1) & 1, gh = w & 1;
    wo_off = (u32)((32 * kq + 16 * gh + 8 * gl + wrow) * D_ + 32 * sec + 16 * nt2 + 8 * p2) * 2u;
  }
  const unsigned char* wob = (const unsigned char*)(wo + 64 * j2);
  auto wo_dma = [&](int q, int base) { dma16(wo_off, wob + (size_t)(64 * q) * D_ * 2, lds0 + base + q * 8192 + w * 1024); };
  const u32 ofo = (u32)((((4 * c2 + m) * 16 + 8 * kh) * 64 + lane) * 16);
  v4u of[8];
#pragma unroll
  for (int q = 0; q < 4; ++q) wo_dma(q, B_WO0_OFF);
  const int trow = 64 * c2 + 8 * w + lc;
  const int tcol = 64 * j2 + 8 * lr;
  const u32 xfo = (u32)((((4 * c2 + m) * 64 + 8 * j2 + lr) * 16 + 8 * kh + lc) * 16);
  v4u xres, wrv, wra;
  st16c(rs, WS_O1 + of_off(8 * c + w, j, lane), attn_tile(lds + B_FR_OFF, w, c == 0 && w == 0, lane));
  BAR();
  const int wfr0 = kh * 32768 + wf_lane_off(lane, 0), wfr1 = kh * 32768 + wf_lane_off(lane, 1);
  const int exr = (((w & ~1) * 4 + (lr >> 1)) * 64 + (2 * (lr & 1)) * 16 + 8 * kh + lc) * 16;
  auto wo_fin = [&](const f32x4 (&acc)[4], v4u res, int exb) {
#pragma unroll
    for (int n = 0; n < 4; ++n) *(LAS f32x4*)(lds + exb + ((w * 4 + n) * 64 + lane) * 16) = acc[n];
    BAR();
    const f32x4 a0 = *(const LAS f32x4*)(lds + exb + exr) + *(const LAS f32x4*)(lds + exb + exr + 4096);
    const f32x4 a1 = *(const LAS f32x4*)(lds + exb + exr + 256) + *(const LAS f32x4*)(lds + exb + exr + 4096 + 256);
    return add8(res, v4u{pack2(a0[0], a0[1]), pack2(a0[2], a0[3]), pack2(a1[0], a1[1]), pack2(a1[2], a1[3])});
  };
  auto red8 = [&](float v) { v += dpp_f<DPP_ROR8>(v); return xor32_add(xor16_add(v)); };
  auto publish_x = [&](auto LC, v4u xv) {
    constexpr int L = decltype(LC)::value;
    st16c(rs, (L ? WS_X3 : WS_X1) + xfo, xv);
    float s1 = 0.f, q1 = 0.f, d1 = 0.f;
#pragma unroll
    for (int e = 0; e < 4; ++e) { const u32 p = xv[e]; s1 = dot2(p, 0x3F803F80u, s1); q1 = dot2(p, p, q1); d1 = dot2(p, wrv[e], d1); }
    s1 = red8(s1); q1 = red8(q1); d1 = red8(d1);
    WAIT_VM(0);
    if (lane < 8) st16c(rs, WS_PS + L * 262144 + (u32)((((8 * c2 + w) * 8 + j2) * 8 + lane) * 16), seal(__float_as_uint(s1), __float_as_uint(q1), __float_as_uint(d1), tag));
  };
  typedef std::integral_constant<int, 0> I0;
  typedef std::integral_constant<int, 1> I1;
  v4u xt;
  v4u ffr[7];
#pragma unroll
  for (int q = 4; q < 8; ++q) wo_dma(q, B_WO0_OFF);
  xres = *(const v4u*)(x + (size_t)trow * D_ + tcol);
  wrv = *(const v4u*)(wr + tcol);
  wra = *(const v4u*)(wr + 8 * lane);
  const u32 flo = WS_FL + (u32)(((c2 >> 1) * 16 + 8 * kh + lc) * 128);
  u32 flv = ld4c(rs, flo);
  asm volatile("" :: "v"(pf[0]), "v"(pf[1]), "v"(pf[2]), "v"(pf[3]), "v"(flv));
  WAIT_VM(0);
  BAR();
  {
    v4u wf2[8][4];
#pragma unroll
    for (int u = 0; u < 8; ++u)
#pragma unroll
      for (int n = 0; n < 4; ++n) wf2[u][n] = wfrag_at(lds + B_WO0_OFF + ((n & 1) ? wfr1 : wfr0) + u * 4096 + (n >> 1) * 2048);
    SCHED_FENCE();
    for (int it = 0; !wave_all(flv == tag) && it < SPIN_MAX; ++it) {
      __builtin_amdgcn_s_sleep(POLL_SLEEP);
      CFENCE();
      flv = ld4c(rs, flo);
    }
    for (int it = 0;; ++it) {
      CFENCE();
      u32 mx = 0;
#pragma unroll
      for (int q = 0; q < 8; ++q) of[q] = ld16c(rs, WS_O1 + ofo + 1024 * q);
#pragma unroll
      for (int q = 0; q < 8; ++q) mx = max(mx, max4(of[q]));
      if (wave_all(mx != SENT) || it >= SPIN_MAX) break;
      __builtin_amdgcn_s_sleep(POLL_SLEEP);
    }
    BAR();
    f32x4 acc[4];
#pragma unroll
    for (int n = 0; n < 4; ++n) acc[n] = f32x4{0.f, 0.f, 0.f, 0.f};
#pragma unroll
    for (int u = 0; u < 8; ++u)
#pragma unroll
      for (int n = 0; n < 4; ++n) acc[n] = mfma16(wf2[u][n], of[u], acc[n]);
    SCHED_FENCE();
    xt = wo_fin(acc, xres, B_WO0_OFF);
  }
  publish_x(I0{}, xt);
  u32 skey; int srow;
  float wsum;
  {
    float t = 0.f;
#pragma unroll
    for (int e = 0; e < 4; ++e) t = dot2(wra[e], 0x3F803F80u, t);
    wsum = sum64(t);
  }
  auto rowpass = [&](auto LC) {
    constexpr int L = decltype(LC)::value;
    const u32 sm = WS_SM + L * SM_STRIDE;
    const u32 po = WS_PS + L * 262144 + (u32)((8 * dsl + lr) * 1024 + lc * 16);
    v4u pv[8];
    for (int it = 0;; ++it) {
      CFENCE();
#pragma unroll
      for (int p = 0; p < 8; ++p) pv[p] = ld16c(rs, po + p * 128);
      u32 er = 0;
#pragma unroll
      for (int p = 0; p < 8; ++p) er |= seal_err(pv[p], tag);
      if (wave_all(er == 0u) || it >= SPIN_MAX) break;
    }
    float s1 = 0.f, q1 = 0.f, d1 = 0.f;
#pragma unroll
    for (int p = 0; p < 8; ++p) { const u32 px = pv[p].x, py = pv[p].y, pz = pv[p].z; s1 += __uint_as_float(px); q1 += __uint_as_float(py); d1 += __uint_as_float(pz); }
    const int row = 64 * dsl + lane;
    const float mean = s1 * (1.0f / D_);
    const float var = fmaxf(__builtin_fmaf(-mean, mean, q1 * (1.0f / D_)), 0.f);
    const u32 st = pack2(mean, __builtin_amdgcn_sqrtf(rbf(rbf(var) + 1e-5f)));
    const float mb = bf_lo(st), r1 = __builtin_amdgcn_rcpf(bf_hi(st));
    st8c(rs, sm + SM_KEY + row * 8, v2u{rkey(rbf(__builtin_fmaf(-mb, wsum, d1) * r1), row), tag});
    float A = 0.f, B = 0.f;
    if (L == 0) {
      const float sc = 1.0f + r1;
      const float mb2 = rbf(__builtin_fmaf(mean, sc, -mb * r1));
      const float rs2 = __builtin_amdgcn_rcpf(rbf(__builtin_amdgcn_sqrtf(rbf(rbf(var * sc * sc) + 1e-5f))));
      A = sc * rs2; B = __builtin_fmaf(mb, r1, mb2) * rs2;
    }
    st16c(rs, sm + st_off(row), seal(st, __float_as_uint(A), __float_as_uint(B), tag));
  };
  auto top8 = [&](auto LC) {
    constexpr int L = decltype(LC)::value;
    const u32 sm = WS_SM + L * SM_STRIDE;
    {
      if (tid < 128) *(LAS u32*)(lds + P_TAB + tid * 4) = 0u;
      if (tid == 128) *(LAS u32*)(lds + P_CNT) = 0u;
      v4u ka;
      for (int it = 0;; ++it) {
        CFENCE();
        const v4u k0 = ld16c(rs, sm + SM_KEY + (lr * 8 + w) * 128 + lc * 16), k1 = ld16c(rs, sm + SM_KEY + 8192 + (lr * 8 + w) * 128 + lc * 16);
        ka = v4u{k0.x, k0.z, k1.x, k1.z};
        if (wave_all(((k0.y ^ tag) | (k0.w ^ tag) | (k1.y ^ tag) | (k1.w ^ tag)) == 0u) || it >= SPIN_MAX) break;
        __builtin_amdgcn_s_sleep(KEY_SLEEP);
      }
      const u32 ma = max64u(max(max(ka.x, ka.y), max(ka.z, ka.w)));
      if (lane == 0) *(LAS u32*)(lds + P_WM + w * 4) = ma;
      BAR();
      int cnt = 0;
      {
        const v4u m0 = *(const LAS v4u*)(lds + P_WM), m1 = *(const LAS v4u*)(lds + P_WM + 16);
        const u32 T = min(min(min(m0.x, m0.y), min(m0.z, m0.w)), min(min(m1.x, m1.y), min(m1.z, m1.w)));
#pragma unroll
        for (int e = 0; e < 4; ++e) {
          const u32 k = ka[e];
          const u64 mk = __builtin_amdgcn_ballot_w64(k >= T);
          if (mk) {
            const int pos = cnt + (int)__builtin_amdgcn_mbcnt_hi((u32)(mk >> 32), __builtin_amdgcn_mbcnt_lo((u32)mk, 0u));
            if (k >= T && pos < 16) *(LAS u32*)(lds + P_TAB + (w * 16 + pos) * 4) = k;
            cnt += __builtin_popcountll(mk);
          }
        }
        if (cnt > 16 && lane == 0) *(LAS u32*)(lds + P_CNT) = 1u;
      }
      BAR();
      const u32 cand = *(const LAS u32*)(lds + P_TAB + lane * 4), cand2 = *(const LAS u32*)(lds + P_TAB + 256 + lane * 4);
      const u32 own = w < 4 ? cand : cand2;
      const u32 ovf = *(const LAS u32*)(lds + P_CNT);
      if (__builtin_amdgcn_readfirstlane(ovf) == 0u) {
        for (int s = 0; s < cnt; ++s) {
          const u32 kj = __builtin_amdgcn_readlane(own, (w & 3) * 16 + s);
          const int rank = __builtin_popcountll(__builtin_amdgcn_ballot_w64(cand > kj)) + __builtin_popcountll(__builtin_amdgcn_ballot_w64(cand2 > kj));
          if (rank < 8 && lane == 0) *(LAS u32*)(lds + P_SEL + rank * 4) = kj;
        }
      } else {
#pragma unroll 1
        for (int it = 0; it < 8; ++it) {
          const u32 mx = max64u(max(max(ka.x, ka.y), max(ka.z, ka.w)));
          BAR();
          if (lane == 0) *(LAS u32*)(lds + P_WM + w * 4) = mx;
          BAR();
          const v4u a0 = *(const LAS v4u*)(lds + P_WM), a1 = *(const LAS v4u*)(lds + P_WM + 16);
          const u32 gm = max(max(max(a0.x, a0.y), max(a0.z, a0.w)), max(max(a1.x, a1.y), max(a1.z, a1.w)));
#pragma unroll
          for (int e = 0; e < 4; ++e) ka[e] = ka[e] == gm ? 0u : ka[e];
          if (tid == 0) *(LAS u32*)(lds + P_SEL + it * 4) = gm;
        }
      }
      BAR();
    }
    skey = *(const LAS u32*)(lds + P_SEL + (tok & 7) * 4);
    srow = key_idx(skey);
    if (fsl == 0 && tid < 8) st8c(rs, sm + SM_SEL + tid * 8, v2u{skey, tag});
  };
  auto sel_fetch = [&](auto LC) {
    constexpr int L = decltype(LC)::value;
    for (int it = 0;; ++it) {
      CFENCE();
      const v2u v = ld8c(rs, WS_SM + L * SM_STRIDE + SM_SEL + (tok & 7) * 8);
      skey = v.x;
      if (wave_all(v.y == tag) || it >= SPIN_MAX) break;
      __builtin_amdgcn_s_sleep(POLL_SLEEP);
    }
    CFENCE();
    srow = key_idx(skey);
    if (tid < 8) *(LAS u32*)(lds + P_SEL + tid * 4) = skey;
  };
  v4u gx0, gx1, gri;
  auto gu_load = [&](auto LC) {
    constexpr int L = decltype(LC)::value;
    const u32 xo = (L ? WS_X3 : WS_X1) + (u32)((((srow >> 4) * 16 + 2 * w) * 64 + g * 16 + (srow & 15)) * 16);
    gx0 = ld16c(rs, xo); gx1 = ld16c(rs, xo + 1024);
    gri = ld16c(rs, WS_SM + L * SM_STRIDE + st_off(srow));
  };
  auto gu_comp = [&](auto LC) {
    constexpr int L = decltype(LC)::value;
    const u32 sm = WS_SM + L * SM_STRIDE;
    const v4u O4 = {0x3F803F80u, 0x3F803F80u, 0x3F803F80u, 0x3F803F80u};
    f32x4 a = {0.f, 0.f, 0.f, 0.f};
    a = mfma16(ffr[0], tok < 8 ? gx0 : O4, a); a = mfma16(ffr[1], tok < 8 ? gx1 : O4, a);
    *(LAS f32x4*)(lds + B_RED_OFF + (w * 64 + lane) * 16) = a;
    BAR();
    if (w == 0) {
      const LAS unsigned char* rp = lds + B_RED_OFF + lane * 16;
      const f32x4 s = ((*(const LAS f32x4*)(rp) + *(const LAS f32x4*)(rp + 1024)) + (*(const LAS f32x4*)(rp + 2048) + *(const LAS f32x4*)(rp + 3072))) +
                      ((*(const LAS f32x4*)(rp + 4096) + *(const LAS f32x4*)(rp + 5120)) + (*(const LAS f32x4*)(rp + 6144) + *(const LAS f32x4*)(rp + 7168)));
      if (!wave_all(sealed(gri, tag))) gri = poll_sealed(rs, sm + st_off(srow), tag);
      const u32 gst = gri.x;
      const float mb = bf_lo(gst), rsd = __builtin_amdgcn_rcpf(bf_hi(gst));
      float hv[4];
#pragma unroll
      for (int e = 0; e < 4; ++e) {
        const float t = __builtin_fmaf(-mb, dpp_f<DPP_ROR8>(s[e]), s[e]) * rsd;
        const u32 tb = __float_as_uint(t);
        const v2u sw = __builtin_amdgcn_permlane32_swap(tb, tb, false, false);
        const u32 gxx = sw.x, uy = sw.y;
        const float gg = rbf(__uint_as_float(gxx)), uu = rbf(__uint_as_float(uy));
        hv[e] = rbf(gg * __builtin_amdgcn_rcpf(1.0f + __expf(-gg))) * uu;
      }
      if (g < 2 && tok < 8) st8c(rs, sm + SM_H + fsl * 128 + tok * 16 + g * 8, v2u{pack2(hv[0], hv[1]), pack2(hv[2], hv[3])});
    }
  };
  auto down = [&](auto LC) {
    constexpr int L = decltype(LC)::value;
    const u32 sm = WS_SM + L * SM_STRIDE;
    const int d = dsl;
    v2u xo2 = {0u, 0u}; u32 st = 0;
    if (L == 1 && w == 0) {
      xo2 = ld8c(rs, WS_X3 + (u32)((((srow >> 4) * 64 + 2 * d + (g >> 1)) * 16 + (srow & 15)) * 16 + (g & 1) * 8));
      st = poll_sealed(rs, sm + st_off(srow), tag).x;
    }
    const u32 ho = sm + SM_H + (u32)(((7 * w + (tok >> 3)) * 4 + g) * 128 + (tok & 7) * 16);
    v4u hf[4];
    for (int it = 0;; ++it) {
      CFENCE();
#pragma unroll
      for (int u = 0; u < 3; ++u) hf[u] = ld16c(rs, ho + u * 1024);
      hf[3] = ld16c(rs, ho + 3072 - (tok >> 3) * 512);
      u32 mx = 0;
#pragma unroll
      for (int u = 0; u < 4; ++u) mx = max(max(mx, max(hf[u].x, hf[u].y)), max(hf[u].z, hf[u].w));
      if (wave_all(mx != SENT) || it >= SPIN_MAX) break;
    }
    f32x4 a0 = {0.f, 0.f, 0.f, 0.f}, a1 = {0.f, 0.f, 0.f, 0.f};
#pragma unroll
    for (int u = 0; u < 7; ++u) { if (u & 1) a1 = mfma16(ffr[u], hf[u >> 1], a1); else a0 = mfma16(ffr[u], hf[u >> 1], a0); }
    *(LAS f32x4*)(lds + B_RED_OFF + (w * 64 + lane) * 16) = tok < 8 ? a0 : a1;
    BAR();
    if (w == 0) {
      const LAS unsigned char* rp = lds + B_RED_OFF + lane * 16;
      f32x4 s = ((*(const LAS f32x4*)(rp) + *(const LAS f32x4*)(rp + 1024)) + (*(const LAS f32x4*)(rp + 2048) + *(const LAS f32x4*)(rp + 3072))) +
                ((*(const LAS f32x4*)(rp + 4096) + *(const LAS f32x4*)(rp + 5120)) + (*(const LAS f32x4*)(rp + 6144) + *(const LAS f32x4*)(rp + 7168)));
      const float sg = rbf(__builtin_amdgcn_rcpf(1.0f + __expf(-key_val(skey))));
      float y[4];
#pragma unroll
      for (int e = 0; e < 4; ++e) y[e] = rbf(rbf(s[e] + dpp_f<DPP_ROR8>(s[e])) * sg);
      if (tok < 8) {
        if (L == 0) st8c(rs, sm + SM_U + (u32)(d * 256 + tok * 32 + g * 8), v2u{pack2(y[0], y[1]), pack2(y[2], y[3])});
        else {
          const u32 a = xo2.x, bb = xo2.y;
          const float mb = bf_lo(st), rsd = __builtin_amdgcn_rcpf(bf_hi(st));
          const float xv[4] = {bf_lo(a), bf_hi(a), bf_lo(bb), bf_hi(bb)};
          float o[4];
#pragma unroll
          for (int e = 0; e < 4; ++e) o[e] = xv[e] + rbf(rbf(rbf(xv[e] - mb) * rsd) + y[e]);
          *(v2u*)(out + (size_t)srow * D_ + 16 * d + 4 * g) = v2u{pack2(o[0], o[1]), pack2(o[2], o[3])};
        }
      }
    }
  };
  {
    const u32 sm = WS_SM;
    const int row0 = 128 * c;
    v4u xl[2][8];
    const u32 xo0 = WS_X1 + (u32)((((8 * c + w) * 16 + 8 * kh) * 64 + lane) * 16), xo1 = WS_X1 + (u32)((((8 * c + (w ^ 1)) * 16 + 8 * kh) * 64 + lane) * 16);
    if (is_dn && w == 0) rowpass(I0{});
    BAR();
    if (is_dn) {
      const u16* p = wd + (size_t)(224 * w + (lane >> 1)) * D_ + 16 * dsl + 8 * (lane & 1);
#pragma unroll
      for (int u = 0; u < 7; ++u) fw[u] = *(const v4u*)(p + (size_t)(32 * u) * D_);
    } else {
      fw[0] = *(const v4u*)(wg + (size_t)(64 * w + lane) * F_ + 8 * fsl);
      fw[1] = *(const v4u*)(wu + (size_t)(64 * w + lane) * F_ + 8 * fsl);
    }
    {
      const int thl = c == 0 ? 0 : 8 * c - 1;
      const int ln = lane < 16 ? ((2 * (8 * c + 2 * m) + (lane >> 2)) * 8 + 4 * kh + (lane & 3)) : ((2 * thl + (lane & 1)) * 8 + w);
      (void)poll_sealed(rs, WS_PS + (u32)(ln * 128), tag);
    }
#pragma unroll
    for (int u = 0; u < XH; ++u) { xl[0][u] = ld16c(rs, xo0 + u * 1024); xl[1][u] = ld16c(rs, xo1 + u * 1024); }
    v4u hl[2];
    {
      const u32 ho = WS_X1 + (u32)(((c == 0 ? 0 : 8 * c - 1) * 1024 + 128 * w + lane) * 16);
      hl[0] = ld16c(rs, ho); hl[1] = ld16c(rs, ho + 1024);
    }
    SCHED_FENCE();
    f32x4 acc[2][6];
#pragma unroll
    for (int n = 0; n < 6; ++n) { acc[0][n] = f32x4{0.f, 0.f, 0.f, 0.f}; acc[1][n] = f32x4{0.f, 0.f, 0.f, 0.f}; }
    v4u wf[2][6];
    const LAS unsigned char* wk = lds + kh * (8 * 6144);
    auto wrd = [&](int ks, int n) { wf[ks & 1][n] = wfrag_at(wk + ((n & 1) ? rb1 : rb0) + ks * 6144 + (n >> 1) * 2048); };
    auto gemm = [&](int k0, int k1) {
      if (k0 >= k1) return;
#pragma unroll
      for (int n = 0; n < 6; ++n) wrd(k0, n);
#pragma unroll
      for (int ks = k0; ks < k1; ++ks) {
#pragma unroll
        for (int n = 0; n < 6; ++n) {
          SCHED_FENCE();
          if (ks + 1 < k1) wrd(ks + 1, n);
#pragma unroll
          for (int t = 0; t < 2; ++t) {
            if (n < 4) acc[t][n] = mfma16(wf[ks & 1][n], xl[t][ks], acc[t][n]);
            else acc[t][n] = mfma16(xl[t][ks], wf[ks & 1][n], acc[t][n]);
          }
        }
      }
      SCHED_FENCE();
    };
    LAS unsigned char* sc = lds + B_FR_OFF + w * 2048;
    const int rdo = g * 256 + ((lane >> 2) & 3) * 32 + (lane & 3) * 8;
    if (!is_dn) {
      *(LAS v4u*)(sc + lane * 32) = fw[0]; *(LAS v4u*)(sc + lane * 32 + 16) = fw[1];
#pragma unroll
      for (int u = 0; u < 2; ++u) { const v2u lo = tr_read(sc + u * 1024 + rdo), hi = tr_read(sc + u * 1024 + rdo + 128); ffr[u] = v4u{lo.x, lo.y, hi.x, hi.y}; }
    } else {
#pragma unroll
      for (int u = 0; u < 7; ++u) {
        *(LAS v4u*)(sc + (u & 1) * 1024 + lane * 16) = fw[u];
        const v2u lo = tr_read(sc + (u & 1) * 1024 + rdo), hi = tr_read(sc + (u & 1) * 1024 + rdo + 128);
        ffr[u] = v4u{lo.x, lo.y, hi.x, hi.y};
      }
    }
    *(LAS v4u*)(lds + B_HX_OFF + w * 2048 + lane * 16) = hl[0];
    *(LAS v4u*)(lds + B_HX_OFF + w * 2048 + 1024 + lane * 16) = hl[1];
    gemm(0, G0);
    if (!is_dn) { top8(I0{}); gu_load(I0{}); }
    v4u tri = ld16c(rs, sm + st_off(trow));
    gemm(G0, G1);
    if (!is_dn) gu_comp(I0{});
#pragma unroll
    for (int u = XH; u < 8; ++u) { xl[0][u] = ld16c(rs, xo0 + u * 1024); xl[1][u] = ld16c(rs, xo1 + u * 1024); }
    SCHED_FENCE();
    gemm(G1, G2);
    {
      if (w >= 4 && w < 7) {
        int rr = 64 * (w - 4) + lane; rr = rr > 143 ? 143 : rr;
        int row = row0 - 16 + rr; row = row < 0 ? 0 : row;
        const v4u ri = poll_sealed(rs, sm + st_off(row), tag);
        *(LAS v2u*)(lds + P_AB + rr * 8) = v2u{ri.y, ri.z};
      } else if (w == 7 && lane < 24) *(LAS f32x4*)(lds + P_CS + lane * 16) = *(const f32x4*)(ws + WS_CS + b * 512 + lane * 16);
    }
    gemm(G2, 8);
    if (is_dn) BAR();
    f32x4 ah;
    {
      f32x4 ah0 = {0.f, 0.f, 0.f, 0.f}, ah1 = {0.f, 0.f, 0.f, 0.f};
      const LAS unsigned char* wp = wk + B_W_OFF + (1 + (m >> 1)) * 2048 + wf_lane_off(lane, m & 1);
      v4u hx[8], hwf[8];
#pragma unroll
      for (int u = 0; u < 8; ++u) { hx[u] = *(const LAS v4u*)(lds + B_HX_OFF + (8 * kh + u) * 1024 + lane * 16); hwf[u] = wfrag_at(wp + u * 6144); }
      if (m < 2) {
#pragma unroll
        for (int u = 0; u < 4; ++u) { ah0 = mfma16(hwf[2 * u], hx[2 * u], ah0); ah1 = mfma16(hwf[2 * u + 1], hx[2 * u + 1], ah1); }
      } else {
#pragma unroll
        for (int u = 0; u < 4; ++u) { ah0 = mfma16(hx[2 * u], hwf[2 * u], ah0); ah1 = mfma16(hx[2 * u + 1], hwf[2 * u + 1], ah1); }
      }
      ah = ah0 + ah1;
    }
    SCHED_FENCE();
    BAR();
#pragma unroll
    for (int n = 0; n < 6; ++n) *(LAS f32x4*)(lds + A_EX_OFF + ((w * 6 + n) * 64 + lane) * 16) = acc[1][n];
    if (kh == 1) *(LAS f32x4*)(lds + A_EXH_OFF + (m * 64 + lane) * 16) = ah;
    BAR();
#pragma unroll
    for (int n = 0; n < 6; ++n) acc[0][n] += *(const LAS f32x4*)(lds + A_EX_OFF + (((w ^ 1) * 6 + n) * 64 + lane) * 16);
    if (kh == 0) ah += *(const LAS f32x4*)(lds + A_EXH_OFF + (m * 64 + lane) * 16);
    if (is_dn) { BAR(); sel_fetch(I0{}); down(I0{}); }
    f32x4 csq[4]; float csv[2];
#pragma unroll
    for (int n = 0; n < 4; ++n) csq[n] = *(const LAS f32x4*)(lds + P_CS + (16 * n + 4 * g) * 4);
#pragma unroll
    for (int n = 0; n < 2; ++n) csv[n] = *(const LAS float*)(lds + P_CS + (64 + 16 * n + tok) * 4);
    const v2u ab = *(const LAS v2u*)(lds + P_AB + (16 + 16 * w + tok) * 8), hab = *(const LAS v2u*)(lds + P_AB + tok * 8);
    const v4u abv0 = *(const LAS v4u*)(lds + P_AB + (16 + 16 * w + 4 * g) * 8), abv1 = *(const LAS v4u*)(lds + P_AB + (16 + 16 * w + 4 * g + 2) * 8);
    const v4u habv0 = *(const LAS v4u*)(lds + P_AB + (4 * g) * 8), habv1 = *(const LAS v4u*)(lds + P_AB + (4 * g + 2) * 8);
    const int rel = srow - row0;
    const bool wg_aff = __builtin_amdgcn_ballot_w64(rel >= -14 && rel < 128) != 0ull;
    const int frow = key_idx(*(const LAS u32*)(lds + P_SEL + w * 4));
    v4u x1v = S4, fri = S4, uv = S4;
    if (wg_aff) {
      x1v = ld16c(rs, WS_X1 + (u32)((((frow >> 4) * 64 + lane) * 16 + (frow & 15)) * 16));
      fri = ld16c(rs, sm + st_off(frow));
      uv = ld16c(rs, sm + SM_U + (u32)((lane >> 1) * 256 + w * 32 + (lane & 1) * 16));
    }
    if (!is_dn) BAR();
    {
      auto fq = [&](f32x4 a, f32x4 cs, v2u p) {
        const u32 px = p.x, py = p.y;
        const f32x4 v = a * __uint_as_float(px) - cs * __uint_as_float(py);
        return v2u{pack2(v[0], v[1]), pack2(v[2], v[3])};
      };
      auto fv = [&](f32x4 a, float cs, v4u p0, v4u p1) {
        const u32 a0 = p0.x, b0 = p0.y, a1 = p0.z, b1 = p0.w, a2 = p1.x, b2 = p1.y, a3 = p1.z, b3 = p1.w;
        return v2u{pack2(a[0] * __uint_as_float(a0) - cs * __uint_as_float(b0), a[1] * __uint_as_float(a1) - cs * __uint_as_float(b1)),
                   pack2(a[2] * __uint_as_float(a2) - cs * __uint_as_float(b2), a[3] * __uint_as_float(a3) - cs * __uint_as_float(b3))};
      };
      LAS unsigned char* frw = lds + B_FR_OFF + (w + 1) * 3072 + lane * 8;
#pragma unroll
      for (int n = 0; n < 4; ++n) *(LAS v2u*)(frw + n * 512) = fq(acc[0][n], csq[n], ab);
#pragma unroll
      for (int n = 0; n < 2; ++n) *(LAS v2u*)(frw + (4 + n) * 512) = fv(acc[0][4 + n], csv[n], abv0, abv1);
      if (kh == 0) {
        v2u pk;
        if (m < 2) pk = fq(ah, m == 0 ? csq[2] : csq[3], hab); else pk = fv(ah, m == 2 ? csv[0] : csv[1], habv0, habv1);
        *(LAS v2u*)(lds + B_FR_OFF + (2 + m) * 512 + lane * 8) = pk;
      }
    }
    bool dma_done = true;
    int hit = -1;
    v4u rxn;
    auto residual = [&] {
#pragma unroll
      for (int t = 0; t < 8; ++t) if (key_idx(*(const LAS u32*)(lds + P_SEL + t * 4)) == trow) hit = t;
      if (!wave_all(sealed(tri, tag))) tri = poll_sealed(rs, sm + st_off(trow), tag);
      rxn = norm8s(xt, tri.x);
      xres = add8(xt, rxn);
      PIN4(rxn); PIN4(xres); PIN4(hit);
    };
    if (wg_aff) {
      v4u fwf[16];
      if (w < 6) {
        const LAS unsigned char* wp = lds + B_W_OFF + (w >> 1) * 2048 + wf_lane_off(lane, w & 1);
#pragma unroll
        for (int ks = 0; ks < 16; ++ks) fwf[ks] = wfrag_at(wp + ks * 6144);
      }
      BAR();
      asm volatile("" :: "v"(x1v), "v"(fri), "v"(uv));
#pragma unroll
      for (int q = 0; q < 8; ++q) wo_dma(q, B_WO1_OFF);
      if (!sealed(fri, tag)) fri = poll_sealed(rs, sm + st_off(frow), tag);
      v4u xn1 = norm8s(x1v, fri.x);
      PIN4(xn1);
      if (!wave_all(ok4(uv))) {
        residual();
        uv = poll16(rs, sm + SM_U + (u32)((lane >> 1) * 256 + w * 32 + (lane & 1) * 16));
      } else dma_done = false;
      const v4u x2v = add8(x1v, add8(xn1, uv));
      *(LAS v4u*)(lds + B_FX_OFF + lr * 2048 + w * 128 + ((lc ^ ((w >> 1) & 7)) * 16)) = norm8s(x2v, row_stats(x2v));
      BAR();
      if (w < 6) {
        f32x4 a0 = {0.f, 0.f, 0.f, 0.f}, a1 = {0.f, 0.f, 0.f, 0.f};
        const LAS unsigned char* xp = lds + B_FX_OFF + tok * 128;
        v4u xa[16];
#pragma unroll
        for (int ks = 0; ks < 16; ++ks) xa[ks] = *(const LAS v4u*)(xp + (ks >> 1) * 2048 + (((4 * (ks & 1) + g) ^ fsw) * 16));
        SCHED_FENCE();
#pragma unroll
        for (int q = 0; q < 8; ++q) { a0 = mfma16(fwf[2 * q], xa[2 * q], a0); a1 = mfma16(fwf[2 * q + 1], xa[2 * q + 1], a1); }
        const f32x4 a = a0 + a1;
        const int pr = rel + 16;
        if (tok < 8 && pr >= 0 && pr < 144) {
          LAS unsigned char* fb = lds + B_FR_OFF + (pr >> 4) * 3072 + w * 512;
          const int st = pr & 15;
          const u32 p0 = pack2(a[0], a[1]), p1 = pack2(a[2], a[3]);
          if (w < 4) *(LAS v2u*)(fb + (g * 16 + st) * 8) = v2u{p0, p1};
          else {
            LAS u16* vb = (LAS u16*)(fb + ((st >> 2) * 16 + 4 * g) * 8 + (st & 3) * 2);
            vb[0] = (u16)p0; vb[4] = (u16)(p0 >> 16); vb[8] = (u16)p1; vb[12] = (u16)(p1 >> 16);
          }
        }
      }
      BAR();
    } else {
      BAR();
#pragma unroll
      for (int q = 0; q < 8; ++q) wo_dma(q, B_WO1_OFF);
      residual();
    }
    st16c(rs, WS_O2 + of_off(8 * c + w, j, lane), attn_tile(lds + B_FR_OFF, w, c == 0 && w == 0, lane));
    if (!dma_done) residual();
    {
      if (__builtin_amdgcn_ballot_w64(hit >= 0) != 0ull) {
        const v4u uv = poll16(rs, sm + SM_U + (u32)((tcol >> 4) * 256 + (hit < 0 ? 0 : hit) * 32 + (lr & 1) * 16));
        if (hit >= 0) xres = add8(xt, add8(rxn, uv));
      }
      WAIT_VM(0);
      BAR();
      v4u wf2[8][4];
#pragma unroll
      for (int u = 0; u < 8; ++u)
#pragma unroll
        for (int n = 0; n < 4; ++n) wf2[u][n] = wfrag_at(lds + B_WO1_OFF + ((n & 1) ? wfr1 : wfr0) + u * 4096 + (n >> 1) * 2048);
      SCHED_FENCE();
      for (int it = 0;; ++it) {
        CFENCE();
        u32 mx = 0;
#pragma unroll
        for (int q = 0; q < 8; ++q) of[q] = ld16c(rs, WS_O2 + ofo + 1024 * q);
#pragma unroll
        for (int q = 0; q < 8; ++q) mx = max(mx, max4(of[q]));
        if (wave_all(mx != SENT) || it >= SPIN_MAX) break;
        __builtin_amdgcn_s_sleep(O_SLEEP);
      }
      f32x4 acc2[4];
#pragma unroll
      for (int n = 0; n < 4; ++n) acc2[n] = f32x4{0.f, 0.f, 0.f, 0.f};
#pragma unroll
      for (int u = 0; u < 8; ++u)
#pragma unroll
        for (int n = 0; n < 4; ++n) acc2[n] = mfma16(wf2[u][n], of[u], acc2[n]);
      xt = wo_fin(acc2, xres, B_EX1_OFF);
      publish_x(I1{}, xt);
    }
  }
  if (is_dn) { if (w == 0) rowpass(I1{}); BAR(); sel_fetch(I1{}); BAR(); }
  else { top8(I1{}); gu_load(I1{}); }
  v4u ori = ld16c(rs, WS_SM + SM_STRIDE + st_off(trow));
  if (b == 255 && tid == 0) st4c(rs, WS_EP, tag);
  if (!is_dn) gu_comp(I1{});
  {
    if (!wave_all(sealed(ori, tag))) ori = poll_sealed(rs, WS_SM + SM_STRIDE + st_off(trow), tag);
    const u32 fst = ori.x;
    bool hit = false;
#pragma unroll
    for (int t = 0; t < 8; ++t) hit = hit || key_idx(*(const LAS u32*)(lds + P_SEL + t * 4)) == trow;
    const v4u o = add8(xt, norm8s(xt, fst));
    const rsrc_t ro = __builtin_amdgcn_make_buffer_rsrc(out, 0, S_ * D_ * 2, 0x00020000);
    if (!hit) __builtin_amdgcn_raw_buffer_store_b128(o, ro, (u32)(trow * D_ + tcol) * 2u, 0, OUT_WT);
  }
  if (is_dn) down(I1{});
}

extern "C" void kernel_launch(void* const* d_in, const int* in_sizes, int n_in, void* d_out, int out_size, void* d_ws, size_t ws_size,
                              hipStream_t stream) {
  const u16* x = (const u16*)d_in[0];
  const u16* wqkv = (const u16*)d_in[1];
  const u16* wo = (const u16*)d_in[2];
  const u16* wr = (const u16*)d_in[4];
  const u16* wg = (const u16*)d_in[5];
  const u16* wu = (const u16*)d_in[6];
  const u16* wd = (const u16*)d_in[7];
  unsigned char* ws = (unsigned char*)d_ws;
  (void)hipFuncSetAttribute((const void*)k_b, hipFuncAttributeMaxDynamicSharedMemorySize, B_LDS);
  k_b<<<256, 512, B_LDS, stream>>>(x, wqkv, wo, wr, wg, wu, wd, (u16*)d_out, ws);
}
```
